```python
import torch

def _softmax(x, axis=-1):
    return torch.softmax(x, dim=axis)

def _gelu(x):
    return torch.nn.functional.gelu(x, approximate='tanh')

def _silu(x):
    return torch.nn.functional.silu(x)

def _sigmoid(x):
    return torch.sigmoid(x)

def _layernorm(x, axis=-1, eps=1e-05):
    m = x.mean(dim=axis, keepdim=True)
    v = x.var(dim=axis, keepdim=True, unbiased=False)
    return (x - m) / torch.sqrt(v + eps)

def _scatter_add(tgt, idx, src, axis=0):
    return tgt.clone().index_add_(axis, idx, src)

def _segment_sum(src, seg, n):
    out = torch.zeros((n,) + src.shape[1:], dtype=src.dtype)
    return out.index_add_(0, seg, src)

def _pad(x, pw):
    flat = tuple((v for pair in reversed(pw) for v in pair))
    return torch.nn.functional.pad(x, flat)
kernel_name = 'recurrent_scan__memory_aug_recurrent_mem_tr__glu_gated_bilinear__prompt'

def setup_inputs():

    _w = lambda a: (lambda t: t.to(torch.bfloat16) if t.is_floating_point() else t)(torch.as_tensor(a))

    def _A_setup():
        B, S, Dm = (1, 2048, 640)
        H, Dh = (32, 192)
        M, seg = (8, 32)
        seg = min(seg, S)
        S = S // seg * seg
        return {'x': _w((lambda *s: torch.randn(*s, dtype=torch.bfloat16) * 0.1)(B, S, Dm)), 'mem0': _w((lambda *s: torch.randn(*s, dtype=torch.bfloat16) * 1.6)(B, M, Dm)), 'Wq': _w((lambda *s: torch.randn(*s, dtype=torch.bfloat16) * 0.8)(Dm, H, Dh) * Dm ** (-0.5)), 'Wk': _w((lambda *s: torch.randn(*s, dtype=torch.bfloat16) * 0.8)(Dm, H, Dh) * Dm ** (-0.5)), 'Wv': _w((lambda *s: torch.randn(*s, dtype=torch.bfloat16) * 1.6)(Dm, H, Dh) * Dm ** (-0.5)), 'Wo': _w((lambda *s: torch.randn(*s, dtype=torch.bfloat16) * 1.6)(H, Dh, Dm) * (H * Dh) ** (-0.5)), 'seg_len': seg}
    _A_in = _A_setup()

    def _B_setup():
        B, S, D, F = (1, 2048, 640, 1536)
        return {'x': _w((lambda *s: torch.randn(*s, dtype=torch.bfloat16) * 0.1)(B, S, D)), 'w_gate': _w((lambda *s: torch.randn(*s, dtype=torch.bfloat16) * 0.4)(D, F) * D ** (-0.5)), 'w_up': _w((lambda *s: torch.randn(*s, dtype=torch.bfloat16) * 0.4)(D, F) * D ** (-0.5)), 'w_down': _w((lambda *s: torch.randn(*s, dtype=torch.bfloat16) * 0.4)(F, D) * F ** (-0.5))}
    _B_in = _B_setup()
    return {'A_x': _A_in['x'], 'A_mem0': _A_in['mem0'], 'A_Wq': _A_in['Wq'], 'A_Wk': _A_in['Wk'], 'A_Wv': _A_in['Wv'], 'A_Wo': _A_in['Wo'], 'A_seg_len': _A_in['seg_len'], 'B_x': _B_in['x'], 'B_w_gate': _B_in['w_gate'], 'B_w_up': _B_in['w_up'], 'B_w_down': _B_in['w_down']}

def reference(A_x, A_mem0, A_Wq, A_Wk, A_Wv, A_Wo, A_seg_len, B_x, B_w_gate, B_w_up, B_w_down):

    def _A_ref(x, mem0, Wq, Wk, Wv, Wo, seg_len):
        B, S, Dm = x.shape
        H, Dh = (Wq.shape[1], Wq.shape[2])
        M = mem0.shape[1]
        scale = Dh ** (-0.5)
        n_seg = S // seg_len
        L = M + seg_len
        idx = torch.arange(L)
        mask = (idx[:, None] < M) | (idx[:, None] >= idx[None, :])
        mem = mem0
        outs = []
        for s in range(n_seg):
            xs = x[:, s * seg_len:(s + 1) * seg_len, :].clone()
            h = torch.concatenate([mem, xs], axis=1)
            q = torch.einsum('btd,dhe->bhte', h, Wq)
            k = torch.einsum('btd,dhe->bhte', h, Wk)
            v = torch.einsum('btd,dhe->bhte', h, Wv)
            scores = torch.einsum('bhqe,bhke->bhqk', q, k) * scale
            scores = torch.where(mask[None, None], scores, -1000000000.0)
            p = _softmax(scores, axis=-1)
            o = torch.einsum('bhqk,bhke->bhqe', p, v)
            o = torch.einsum('bhte,hed->btd', o, Wo)
            mem = o[:, :M, :].clone()
            outs.append(o[:, M:, :])
        return torch.concatenate(outs, axis=1)

    def _B_ref(x, w_gate, w_up, w_down):
        g = torch.einsum('bsd,df->bsf', x, w_gate)
        u = torch.einsum('bsd,df->bsf', x, w_up)
        h = g * u
        return torch.einsum('bsf,fd->bsd', h, w_down)
    x = A_x
    xn = _layernorm(x, axis=-1)
    s = _A_ref(xn, A_mem0, A_Wq, A_Wk, A_Wv, A_Wo, A_seg_len)
    g = _silu(_B_ref(xn, B_w_gate, B_w_up, B_w_down))
    return x + s * g
```

```cpp
#include <hip/hip_runtime.h>
#include <hip/hip_bf16.h>
#include <stdint.h>
#include <type_traits>

namespace {

typedef unsigned u32;
typedef unsigned short u16;
typedef unsigned long long u64;
typedef u32 v2u __attribute__((ext_vector_type(2)));
typedef u32 v4u __attribute__((ext_vector_type(4)));
typedef u32 v8u __attribute__((ext_vector_type(8)));
typedef float f32x4 __attribute__((ext_vector_type(4)));
typedef float f32x2 __attribute__((ext_vector_type(2)));
typedef __bf16 bf16x2 __attribute__((ext_vector_type(2)));
typedef __bf16 bf16x8 __attribute__((ext_vector_type(8)));
typedef short s16x4 __attribute__((ext_vector_type(4)));
#define LAS __attribute__((address_space(3)))
#define DEV __device__ __forceinline__

constexpr int D = 640, H = 32, DH = 192, HD = H * DH, NSEG = 64, FF = 1536;
constexpr int NS = 32, NV = 32, NO = 40, NCHAIN = NS + NV + NO;

constexpr size_t MB = 1u << 20;
constexpr size_t OFF_XN = 1 * MB;
constexpr size_t OFF_KF = 4 * MB;
constexpr size_t OFF_QF = 30 * MB;
constexpr size_t OFF_SX = 56 * MB;
constexpr size_t OFF_YF = 65 * MB;
constexpr size_t OFF_KM = 149 * MB;
constexpr size_t OFF_HF = 156 * MB;
constexpr size_t OFF_GT = 163 * MB;
constexpr size_t OFF_PS = 166 * MB;
constexpr size_t OFF_HO = 172 * MB;
constexpr size_t WS_NEED = OFF_HO + 2 * MB + 48 * MB;
constexpr int MBLK = 0, PBLK = 2 * 65, DBLK = PBLK + 3 * 64, TBLK = DBLK + 1, NBLK = TBLK + 40 * 64;
constexpr int NPAGE = (NBLK * 16384 + (1 << 21) - 1) >> 21;
constexpr u32 HO_BYTES = (u32)NPAGE << 21;
static_assert(NPAGE <= 32, "parity mask is 32 bits");
constexpr int CW_TMO = 0, CW_PM = 64;
constexpr u32 SENT = 0xFFFFFFFFu;
constexpr u32 SPIN_CAP = 1u << 21;

struct Params {
  const u16 *x, *mem0, *Wq, *Wk, *Wv, *Wo, *Wg, *Wu, *Wd;
  u16 *out;
  unsigned char *ws;
  unsigned char *ho;
};

typedef __amdgpu_buffer_rsrc_t rsrc_t;
DEV rsrc_t mk_rsrc(const void *p, u32 bytes) { return __builtin_amdgcn_make_buffer_rsrc((void *)p, 0, (int)bytes, 0x00020000); }
DEV v4u ld128c(rsrc_t r, u32 off) { return __builtin_amdgcn_raw_buffer_load_b128(r, (int)off, 0, 16); }
DEV u32 ld32c(rsrc_t r, u32 off) { return __builtin_amdgcn_raw_buffer_load_b32(r, (int)off, 0, 16); }
DEV void st128c(rsrc_t r, u32 off, v4u v) { __builtin_amdgcn_raw_buffer_store_b128(v, r, (int)off, 0, 16); }
DEV void st64c(rsrc_t r, u32 off, v2u v) { __builtin_amdgcn_raw_buffer_store_b64(v, r, (int)off, 0, 16); }
DEV void st32c(rsrc_t r, u32 off, u32 v) { __builtin_amdgcn_raw_buffer_store_b32(v, r, (int)off, 0, 16); }

constexpr u32 OOB = 0x80000000u;
DEV void add1(rsrc_t r, u32 off, bool act) { __builtin_amdgcn_raw_ptr_buffer_atomic_add_i32(1, r, (int)(act ? off : OOB), 0, 0); }
DEV u32 add1r(rsrc_t r, u32 off, bool act) { return (u32)__builtin_amdgcn_raw_ptr_buffer_atomic_add_i32(1, r, (int)(act ? off : OOB), 0, 0); }
DEV void st32c1(rsrc_t r, u32 off, u32 v, bool act) { st32c(r, act ? off : OOB, v); }

DEV u32 pk_bf16(float lo, float hi) {
  f32x2 f = {lo, hi};
  return __builtin_bit_cast(u32, __builtin_convertvector(f, bf16x2));
}
DEV float bf2f(u32 b) { return __builtin_bit_cast(float, b << 16); }
DEV float rbf(float f) { return bf2f(pk_bf16(f, 0.f) & 0xFFFFu); }
DEV u32 nosent(u32 v) { return v < 0xFFFFFFFEu ? v : 0xFFFFFFFEu; }

DEV f32x4 mfma(v4u a, v4u b, f32x4 c) {
  return __builtin_amdgcn_mfma_f32_16x16x32_bf16(__builtin_bit_cast(bf16x8, a), __builtin_bit_cast(bf16x8, b), c, 0, 0, 0);
}
DEV v8u cat8(v4u a, v4u b) { return __builtin_shufflevector(a, b, 0, 1, 2, 3, 4, 5, 6, 7); }
DEV v4u cat4(v2u a, v2u b) { return __builtin_shufflevector(a, b, 0, 1, 2, 3); }

DEV v4u lds128(LAS unsigned char *L, u32 off) { return *(LAS v4u *)(L + off); }
DEV void lds128w(LAS unsigned char *L, u32 off, v4u v) { *(LAS v4u *)(L + off) = v; }
DEV void lds64w(LAS unsigned char *L, u32 off, v2u v) { *(LAS v2u *)(L + off) = v; }
DEV v2u ldstr(LAS unsigned char *L, u32 off) {
  return __builtin_bit_cast(v2u, __builtin_amdgcn_ds_read_tr16_b64_v4i16((LAS s16x4 *)(L + off)));
}
#define WAIT_VM(n) asm volatile("s_waitcnt vmcnt(" #n ")" ::: "memory")
#define WAIT_LGKM0() asm volatile("s_waitcnt lgkmcnt(0)" ::: "memory")
DEV void bar() {
  asm volatile("s_waitcnt lgkmcnt(0)" ::: "memory");
  __builtin_amdgcn_s_barrier();
  asm volatile("" ::: "memory");
}
DEV void bar_raw() {
  asm volatile("" ::: "memory");
  __builtin_amdgcn_s_barrier();
  asm volatile("" ::: "memory");
}
DEV void dma16(const void *base, u32 voff, u32 ldsaddr) {
  u32 keep;
  asm volatile("s_mov_b32 %0, m0\n\ts_mov_b32 m0, %3\n\ts_nop 0\n\tglobal_load_lds_dwordx4 %1, %2\n\ts_mov_b32 m0, %0"
               : "=&s"(keep)
               : "v"(voff), "s"(base), "s"(ldsaddr)
               : "memory");
}
DEV u32 opq(u32 v) { asm volatile("" : "+v"(v)); return v; }
DEV float u2f(u32 v) { return __builtin_bit_cast(float, v); }
DEV u32 f2u(float v) { return __builtin_bit_cast(u32, v); }
DEV float swap32_sum(float a, float b) {
  auto r = __builtin_amdgcn_permlane32_swap(f2u(a), opq(f2u(b)), false, false);
  return u2f(r[0]) + u2f(r[1]);
}
DEV float xsum16(float a) { auto r = __builtin_amdgcn_permlane16_swap(f2u(a), opq(f2u(a)), false, false); return u2f(r[0]) + u2f(r[1]); }
DEV float xsum32(float a) { auto r = __builtin_amdgcn_permlane32_swap(f2u(a), opq(f2u(a)), false, false); return u2f(r[0]) + u2f(r[1]); }

DEV float vmax(float a, float b) { float r; asm("v_max_f32 %0, %1, %2" : "=v"(r) : "v"(a), "v"(b)); return r; }
DEV float vmax3(float a, float b, float c) { float r; asm("v_max3_f32 %0, %1, %2, %3" : "=v"(r) : "v"(a), "v"(b), "v"(c)); return r; }
DEV float rmax16(float a) { auto r = __builtin_amdgcn_permlane16_swap(f2u(a), opq(f2u(a)), false, false); return vmax(u2f(r[0]), u2f(r[1])); }
DEV float rmax32(float a) { auto r = __builtin_amdgcn_permlane32_swap(f2u(a), opq(f2u(a)), false, false); return vmax(u2f(r[0]), u2f(r[1])); }
DEV u32 blk_off(u32 blk, u32 pmask) { return (blk << 14) | (((pmask >> (blk >> 7)) & 1u) << 13); }

struct StageLane { u32 ve; };
DEV StageLane stage_lane(int lane, u32 rowstride_bytes) {
  const int r = lane >> 3, sg = (lane & 7) >> 1, hf = lane & 1, k0 = (lane >> 4) & 1;
  StageLane s;
  s.ve = r * rowstride_bytes + (16 * (sg ^ k0) + 8 * hf) * 2;
  return s;
}
DEV void stage_instr(const unsigned char *src, u32 rowstride_bytes, int m, const StageLane &s, u32 ldsaddr) {
  dma16(src + (size_t)m * 8 * rowstride_bytes, s.ve ^ (u32)((m & 1) << 6), ldsaddr + m * 1024);
}

#define ACLOB "a0","a1","a2","a3","a4","a5","a6","a7","a8","a9","a10","a11","a12","a13","a14","a15","a16","a17","a18","a19","a20","a21","a22","a23","a24","a25","a26","a27","a28","a29","a30","a31","a32","a33","a34","a35","a36","a37","a38","a39","a40","a41","a42","a43","a44","a45","a46","a47","a48","a49","a50","a51","a52","a53","a54","a55","a56","a57","a58","a59","a60","a61","a62","a63","a64","a65","a66","a67","a68","a69","a70","a71","a72","a73","a74","a75","a76","a77","a78","a79","a80","a81","a82","a83","a84","a85","a86","a87","a88","a89","a90","a91","a92","a93","a94","a95","a96","a97","a98","a99","a100","a101","a102","a103","a104","a105","a106","a107","a108","a109","a110","a111","a112","a113","a114","a115","a116","a117","a118","a119","a120","a121","a122","a123","a124","a125","a126","a127","a128","a129","a130","a131","a132","a133","a134","a135","a136","a137","a138","a139","a140","a141","a142","a143","a144","a145","a146","a147","a148","a149","a150","a151","a152","a153","a154","a155","a156","a157","a158","a159","a160","a161","a162","a163","a164","a165","a166","a167","a168","a169","a170","a171","a172","a173","a174","a175","a176","a177","a178","a179","a180","a181","a182","a183","a184","a185","a186","a187","a188","a189","a190","a191","a192","a193","a194","a195","a196","a197","a198","a199","a200","a201","a202","a203","a204","a205","a206","a207","a208","a209","a210","a211","a212","a213","a214","a215","a216","a217","a218","a219","a220","a221","a222","a223","a224","a225","a226","a227","a228","a229","a230","a231","a232","a233","a234","a235","a236","a237","a238","a239","a240","a241","a242","a243","a244","a245","a246","a247","a248","a249","a250","a251","a252","a253","a254","a255"
template <int I, int N, class F> DEV void sfor(F &&f) {
  if constexpr (I < N) { f(std::integral_constant<int, I>{}); sfor<I + 1, N>(f); }
}
template <int N> DEV void smfA(f32x4 &c, const v4u &a, int idx) {
  asm volatile("v_smfmac_f32_16x16x64_bf16 %0, %1, a[%c3:%c4], %2" : "+v"(c) : "v"(a), "v"(idx), "i"(N), "i"(N + 7) : ACLOB);
}
DEV void smfV(f32x4 &c, const v4u &a, const v8u &b, int idx) {
  asm volatile("v_smfmac_f32_16x16x64_bf16 %0, %1, %2, %3" : "+v"(c) : "v"(a), "v"(b), "v"(idx) : ACLOB);
}
DEV void mfmaV0(f32x4 &c, const v4u &a, const v4u &b) { asm volatile("v_mfma_f32_16x16x32_bf16 %0, %1, %2, 0" : "=&v"(c) : "v"(a), "v"(b) : ACLOB); }
DEV void mfmaV(f32x4 &c, const v4u &a, const v4u &b) { asm volatile("v_mfma_f32_16x16x32_bf16 %0, %1, %2, %0" : "+v"(c) : "v"(a), "v"(b) : ACLOB); }
#define MFMA_DONE(c) asm volatile("s_nop 7" : "+v"(c))
template <int N> DEV void trA(u32 addr) {
  asm volatile("ds_read_b64_tr_b16 a[%c1:%c2], %0\n\tds_read_b64_tr_b16 a[%c3:%c4], %0 offset:512" ::"v"(addr), "i"(N), "i"(N + 1), "i"(N + 2), "i"(N + 3)
               : "memory", ACLOB);
}
DEV u32 tr_addr_rt(u32 slot_addr, int lane, int T) {
  const int g = lane >> 4, q = (lane >> 2) & 3, p = lane & 3, key = ((q >> 1) & 1) | ((g & 1) << 1);
  return slot_addr + (T >> 2) * 4096 + (8 * g + q) * 128 + (((T & 3) ^ key) << 5) + 8 * p;
}

template <int N> DEV bool poll(rsrc_t rs, const u32 (&off)[N], v4u (&v)[N]) {
  u32 spins = SPIN_CAP;
  do {
    asm volatile("" ::: "memory");
#pragma unroll
    for (int i = 0; i < N; ++i) v[i] = ld128c(rs, off[i]);
    u32 m = 0;
#pragma unroll
    for (int i = 0; i < N; ++i) m = max(max(m, max(v[i].x, v[i].y)), max(v[i].z, v[i].w));
    if (__builtin_amdgcn_ballot_w64(m == SENT) == 0) return true;
  } while (--spins);
  return false;
}
DEV void db_wait(rsrc_t rs, u32 off, u32 want, bool poller) {
  if (poller) {
    u32 a = ld32c(rs, off), b, c;
    __builtin_amdgcn_sched_barrier(0);
    b = ld32c(rs, off);
    __builtin_amdgcn_sched_barrier(0);
    c = ld32c(rs, off);
    for (u32 spins = 0; spins < 2048; ++spins) {
      if (__builtin_amdgcn_ballot_w64(a < want) == 0) break;
      asm volatile("" ::: "memory");
      a = ld32c(rs, off);
      if (__builtin_amdgcn_ballot_w64(b < want) == 0) break;
      asm volatile("" ::: "memory");
      b = ld32c(rs, off);
      if (__builtin_amdgcn_ballot_w64(c < want) == 0) break;
      asm volatile("" ::: "memory");
      c = ld32c(rs, off);
    }
  }
  bar_raw();
}
DEV bool wait_ge(rsrc_t rs, u32 off, u32 want) {
  for (u32 spins = 0;; ++spins) {
    asm volatile("" ::: "memory");
    const u32 v = __builtin_amdgcn_readfirstlane(ld32c(rs, off));
    if (v >= want) return true;
    if (spins > SPIN_CAP) return false;
    __builtin_amdgcn_s_sleep(4);
  }
}

#define PIN_V(x) asm volatile("" : "+v"(x))
#define SCHED() __builtin_amdgcn_sched_barrier(0)
constexpr u32 CB_KF = 4096;
constexpr u32 CB_Y = CB_KF + 16 * 32 * 128;
constexpr u32 CB_KM = CB_Y + 16 * 128;
constexpr u32 CB_T = CB_KM + 64 * 128;
constexpr u32 CB_H = CB_T + 64 * 128;
constexpr u32 CB_G = CB_H + 16 * 128;
constexpr u32 CB_P = CB_G + 16 * 5 * 128;
constexpr u32 CB_Q = CB_P + 64 * 128;
constexpr u32 CB_END = CB_Q + 8 * 128;
constexpr u32 CTRL_BYTES = 128 * 1024;
static_assert(CB_END <= CTRL_BYTES, "");

DEV void probe_page(const Params &p, int page) {
  if (threadIdx.x == 0) {
    u32 best[2] = {~0u, ~0u};
    for (int it = 0; it < 6; ++it) {
#pragma unroll
      for (int par = 0; par < 2; ++par) {
        u32 *a = (u32 *)(p.ho + ((size_t)page << 21) + par * 8192 + 4096);
        u32 ret;
        const u64 t0 = __builtin_amdgcn_s_memtime();
        asm volatile("s_waitcnt lgkmcnt(0)\n\tglobal_atomic_add %0, %1, %2, off sc0 sc1\n\ts_waitcnt vmcnt(0)" : "=&v"(ret) : "v"(a), "v"(0u) : "memory");
        const u64 t1 = __builtin_amdgcn_s_memtime();
        best[par] = min(best[par], (u32)(t1 - t0) + (ret & 0u));
      }
    }
    st64c(mk_rsrc(p.ws, CTRL_BYTES), (CW_PM + 2 * page) * 4, v2u{best[0] | 1u, best[1] | 1u});
  }
}
DEV u32 page_mask(const Params &p) {
  const rsrc_t r = mk_rsrc(p.ws, CTRL_BYTES);
  const int pg = (threadIdx.x & 63) < NPAGE ? (threadIdx.x & 63) : 0;
  u32 a = 0, b = 0;
  for (u32 spins = 0; spins < SPIN_CAP; ++spins) {
    asm volatile("" ::: "memory");
    const v2u v = __builtin_amdgcn_raw_buffer_load_b64(r, (CW_PM + 2 * pg) * 4, 0, 16);
    a = v.x; b = v.y;
    if (__builtin_amdgcn_ballot_w64(a == 0u || b == 0u) == 0) break;
    __builtin_amdgcn_s_sleep(8);
  }
  return (u32)__builtin_amdgcn_ballot_w64(b < a);
}

constexpr u32 S_MS = 80 * 1024, S_QK = S_MS + 10240, S_SX = 144 * 1024;
DEV u32 s_wslot(int i, int w) {
  return i < 4 ? 48 * 1024 + (w * 4 + i) * 2048 : (i < 10 ? 96 * 1024 + (w * 6 + (i - 4)) * 2048 : (w * 6 + (i - 10)) * 2048);
}
DEV bool get_M(rsrc_t rh, LAS unsigned char *L, u32 ms, int s, u32 pmask, int tid, bool ok, bool poller, v4u (&m)[3]) {
  if (s > 0) db_wait(rh, blk_off(DBLK, pmask) + 32 * 64, s, poller && ok);
  const u32 b0 = blk_off(MBLK + 2 * s, pmask), b1 = blk_off(MBLK + 2 * s + 1, pmask);
  const u32 off[3] = {b0 + tid * 16, b0 + 4096 + tid * 16, b1 + (tid & 127) * 16};
  if (ok) ok = poll<3>(rh, off, m);
  const u32 mw = opq(ms + tid * 16);
  lds128w(L, mw, m[0]);
  lds128w(L, mw + 4096, m[1]);
  lds128w(L, ms + 8192 + (tid & 127) * 16, m[2]);
  return ok;
}
DEV v8u lds_frag(LAS unsigned char *L, u32 off) { return cat8(lds128(L, off), lds128(L, off + 1024)); }

struct KT { int kb, t; };
constexpr int own_kb(int W, int i) { return i == 0 ? W : (i == 1 ? 4 + W : 8 + (W & 1)); }
constexpr int rest_kb(int W, int i) {
  const int order[10] = {0, 9, 1, 2, 8, 3, 4, 7, 5, 6};
  int n = 0;
  for (int k = 0; k < 10; ++k) {
    const int kb = order[k];
    if (kb == own_kb(W, 0) || kb == own_kb(W, 1) || kb == own_kb(W, 2)) continue;
    if (n == i) return kb;
    ++n;
  }
  return 0;
}
constexpr KT s_op(int W, int o) { return o < 18 ? KT{own_kb(W, o % 3), o / 3} : KT{rest_kb(W, ((o - 18) % 14) / 2), 2 * ((o - 18) / 14) + ((o - 18) & 1)}; }
constexpr bool s_lds(KT x) { return x.kb >= 8 || (x.kb == 7 && x.t >= 2); }
constexpr int s_uidx(int W, int o) { int n = 0; for (int i = 0; i < o; ++i) n += s_lds(s_op(W, i)) ? 1 : 0; return n; }
constexpr KT s_uop(int W, int u) { int n = 0; for (int i = 0; i < 60; ++i) { if (s_lds(s_op(W, i))) { if (n == u) return s_op(W, i); ++n; } } return KT{0, 0}; }
template <int W> DEV void s_loop(const Params &p, LAS unsigned char *L, int h, u32 pmask, v8u (&Wr)[12]) {
  constexpr int WK = W == 0 ? 0 : (W == 3 ? 3 : 1), w = W;
  const int tid = threadIdx.x, lane = tid & 63;
  const rsrc_t rh = mk_rsrc(p.ho, HO_BYTES), rw = mk_rsrc(p.ws, (u32)OFF_HO);
  const int idx = (lane & 8) ? 0xEEEE : 0x4444;
  const int n16 = lane & 15, g = lane >> 4;
  const u32 trq = opq(S_QK + (16 * (g >> 1) + 4 * (g & 1) + ((lane >> 2) & 3)) * 16 + (lane & 1) * 8);
  const u32 wl0 = opq(s_wslot(0, w) + lane * 16), wl4 = opq(s_wslot(4, w) + lane * 16), wl10 = opq(s_wslot(10, w) + lane * 16);
  const u32 qkw = opq(S_QK + (w >> 1) * 3072 + (16 * (6 * (w & 1) + (lane >> 5)) + n16) * 16 + (g & 1) * 8);
  const u32 ma = opq(S_MS + lane * 16), sxo = opq(S_SX + n16 * 32);
  const u32 segl = ((h >> 1) & 7) * 1024 + (16 * (2 * (h & 1) + (WK == 1 ? w - 1 : 0)) + n16) * 16 + (2 * (g & 1) + (g >> 1)) * 4;
  const u32 meml = (h >> 3) * 1024 + (16 * ((h & 7) >> 1) + (n16 & 7) + 8 * (g & 1)) * 16 + (h & 1) * 8;
  v4u Kx[6];
  bool ok = true;
  if (WK == 1) {
    ok = wait_ge(rw, CB_KF + (0 * 32 + h) * 128, 1);
#pragma unroll
    for (int ks = 0; ks < 6; ++ks) Kx[ks] = ld128c(rw, (u32)OFF_KF + h * 12288 + ((w - 1) * 6 + ks) * 1024 + lane * 16);
  }
  for (int s = 0; s < NSEG; ++s) {
    f32x4 acc[6];
#pragma unroll
    for (int t = 0; t < 6; ++t) { acc[t] = f32x4{0.f, 0.f, 0.f, 0.f}; PIN_V(acc[t]); }
    const u32 pb = PBLK + 3 * s;
    u32 pdst = WK == 0 ? blk_off(pb + 2, pmask) + meml : blk_off(pb + (h >> 4), pmask) + segl;
    PIN_V(pdst);
    auto lfrag = [&](auto U) {
      constexpr KT x = s_uop(W, U);
      constexpr int li = x.kb == 7 ? x.t - 2 : (x.kb == 8 ? 4 + x.t : 10 + x.t);
      return lds_frag(L, li < 4 ? wl0 + li * 2048 : (li < 10 ? wl4 + (li - 4) * 2048 : wl10 + (li - 10) * 2048));
    };
    v8u lb[4];
    sfor<0, 4>([&](auto U) { lb[U] = lfrag(U); PIN_V(lb[U]); });
    v4u m[3];
    ok = get_M(rh, L, S_MS, s, pmask, tid, ok, W == 3, m);
    if (WK == 0) add1(rw, CB_KM + (s - 1) * 128, lane == 0 && s > 0);
    {
      v4u A[10];
      A[own_kb(W, 0)] = m[0]; A[own_kb(W, 1)] = m[1]; A[own_kb(W, 2)] = m[2];
      float ev[4];
      auto epi = [&](auto PR, auto STEP) {
        constexpr int pr = PR, st = STEP;
        if constexpr (st == 0) { ev[0] = swap32_sum(acc[2 * pr][0], acc[2 * pr + 1][0]); }
        if constexpr (st == 1) { ev[1] = swap32_sum(acc[2 * pr][1], acc[2 * pr + 1][1]); }
        if constexpr (st == 2) { ev[2] = swap32_sum(acc[2 * pr][2], acc[2 * pr + 1][2]); }
        if constexpr (st == 3) { ev[3] = swap32_sum(acc[2 * pr][3], acc[2 * pr + 1][3]); }
        if constexpr (st == 4) lds64w(L, qkw + pr * 512, v2u{pk_bf16(ev[0], ev[1]), pk_bf16(ev[2], ev[3])});
      };
      sfor<0, 60>([&](auto O) {
        constexpr int o = O;
        constexpr KT x = s_op(W, o);
        if constexpr (o == 12) {
          asm volatile("s_waitcnt lgkmcnt(%0)" ::"n"(2 * s_uidx(W, 12)) : "memory");
          bar_raw();
          sfor<0, 7>([&](auto I) { A[rest_kb(W, I)] = lds128(L, ma + rest_kb(W, I) * 1024); });
        }
        if constexpr (!s_lds(x)) {
          constexpr int r = x.kb * 6 + x.t;
          if constexpr (r < 32) smfA<8 * r>(acc[x.t], A[x.kb], idx); else smfV(acc[x.t], A[x.kb], Wr[r - 32], idx);
        } else {
          constexpr int u = s_uidx(W, o);
          smfV(acc[x.t], A[x.kb], lb[u % 4], idx);
          if constexpr (u + 4 < 16) lb[u % 4] = lfrag(std::integral_constant<int, u + 4>{});
        }
        if constexpr (o >= 40 && o < 45) epi(std::integral_constant<int, 0>{}, std::integral_constant<int, o - 40>{});
        if constexpr (o >= 54 && o < 59) epi(std::integral_constant<int, 1>{}, std::integral_constant<int, o - 54>{});
        SCHED();
      });
      asm volatile("s_nop 7" : "+v"(acc[4]), "+v"(acc[5]));
      sfor<0, 5>([&](auto ST) { epi(std::integral_constant<int, 2>{}, ST); });
    }
    bar();
    v4u kf[6];
    float e[4], mw, lw;
    const float sc = 0.07216878364870322f * 1.4426950408889634f;
    if (WK != 3) {
      f32x4 sv;
      v4u qf[6];
#pragma unroll
      for (int ks = 0; ks < 6; ++ks) {
        qf[ks] = cat4(ldstr(L, trq + ks * 512), ldstr(L, trq + ks * 512 + 128));
        if (WK == 0) kf[ks] = cat4(ldstr(L, trq + 3072 + ks * 512), ldstr(L, trq + 3072 + ks * 512 + 128));
      }
      SCHED();
#pragma unroll
      for (int ks = 0; ks < 6; ++ks) {
        if (ks == 0) mfmaV0(sv, WK == 0 ? kf[ks] : Kx[ks], qf[ks]); else mfmaV(sv, WK == 0 ? kf[ks] : Kx[ks], qf[ks]);
      }
      MFMA_DONE(sv);
      if (WK == 0) st32c1(rh, blk_off(DBLK, pmask), s + 1, lane == 0 && h == 0);
      if (WK == 0 && g >= 2) sv = f32x4{-INFINITY, -INFINITY, -INFINITY, -INFINITY};
      mw = rmax32(rmax16(vmax(vmax3(sv[0], sv[1], sv[2]), sv[3])));
#pragma unroll
      for (int r = 0; r < 4; ++r) e[r] = __builtin_amdgcn_exp2f((sv[r] - mw) * sc);
      lw = xsum32(xsum16((e[0] + e[1]) + (e[2] + e[3])));
      *(LAS f32x2 *)(L + sxo + (WK == 0 ? 0 : w) * 8) = f32x2{mw, lw};
    }
    bar();
    if (WK != 3) {
      const f32x4 x01 = *(LAS f32x4 *)(L + sxo);
      const f32x2 x2 = *(LAS f32x2 *)(L + sxo + 16);
      const float mx = vmax3(x01[0], x01[2], x2[0]);
      const float tot = x01[1] * __builtin_amdgcn_exp2f((x01[0] - mx) * sc) + x01[3] * __builtin_amdgcn_exp2f((x01[2] - mx) * sc) +
                        x2[1] * __builtin_amdgcn_exp2f((x2[0] - mx) * sc);
      const float f = __builtin_amdgcn_exp2f((mw - mx) * sc) * __builtin_amdgcn_rcpf(tot);
      const u32 d0 = nosent(pk_bf16(e[0] * f, e[1] * f)), d1 = nosent(pk_bf16(e[2] * f, e[3] * f));
      if (WK == 0) st64c(rh, (n16 < 8 && g < 2) ? pdst : OOB, v2u{d0, d1});
      else st32c(rh, pdst, n16 < 8 ? d0 : d1);
      if (s + 2 < NSEG) {
        if (WK == 0) st64c(rh, (n16 < 8 && g < 2) ? blk_off(pb + 8, pmask) + meml : OOB, v2u{SENT, SENT});
        else st32c(rh, blk_off(pb + 6 + (h >> 4), pmask) + segl, SENT);
      }
    }
    if (WK == 0) {
      const u32 ko = n16 < 8 ? (u32)OFF_KM + (s * 32 + h) * 3072 + g * 128 + n16 * 16 : OOB;
#pragma unroll
      for (int ks = 0; ks < 6; ++ks) st128c(rw, ko + ks * 512, kf[ks]);
    }
    if (WK == 1 && s + 1 < NSEG) {
      if (((s + 1) & 3) == 0) { if (ok) ok = wait_ge(rw, CB_KF + (((s + 1) >> 2) * 32 + h) * 128, 1); }
#pragma unroll
      for (int ks = 0; ks < 6; ++ks)
        Kx[ks] = ld128c(rw, (u32)OFF_KF + ((s + 1) * 32 + h) * 12288 + ((w - 1) * 6 + ks) * 1024 + lane * 16);
    }
  }
  if (WK == 0) {
    WAIT_VM(0);
    add1(rw, CB_KM + (NSEG - 1) * 128, lane == 0);
  }
  st32c1(rw, CW_TMO * 4, 1u, !ok && lane == 0);
}
DEV void role_S(const Params &p, LAS unsigned char *L, int h) {
  const int tid = threadIdx.x, lane = tid & 63, w = __builtin_amdgcn_readfirstlane(tid >> 6);
  const u32 lbase = (u32)(uintptr_t)L;
  v8u Wr[12];
  {
    const StageLane sl = stage_lane(lane, HD * 2);
    auto issue = [&](int ks) {
#pragma unroll
      for (int i = 0; i < 6; ++i) {
        const int n = w * 6 + i, ch = n >> 2, m = n & 3;
        const u16 *Wm = ch < 3 ? p.Wq : p.Wk;
        const unsigned char *src = (const unsigned char *)(Wm + (size_t)ks * 32 * HD + h * DH + (ch % 3) * 64);
        stage_instr(src, HD * 2, m, sl, lbase + (ks % 6) * 24576 + ch * 4096);
      }
    };
#pragma unroll
    for (int ks = 0; ks < 6; ++ks) issue(ks);
    v4u tmp[12];
    sfor<0, 20>([&](auto KS) {
      constexpr int ks = KS, kb = ks >> 1, hf = ks & 1;
      constexpr int later = (ks + 5 <= 19 ? 5 : 19 - ks);
      if (later == 5) WAIT_VM(30); else if (later == 4) WAIT_VM(24); else if (later == 3) WAIT_VM(18);
      else if (later == 2) WAIT_VM(12); else if (later == 1) WAIT_VM(6); else WAIT_VM(0);
      bar();
      sfor<0, 6>([&](auto T) {
        constexpr int t = T, r = kb * 6 + t;
        const u32 ta = tr_addr_rt((ks % 6) * 24576 + ((w >> 1) * 3) * 4096, lane, 6 * (w & 1) + t);
        constexpr bool inreg = kb < 7 || (kb == 7 && t < 2);
        if constexpr (inreg && r < 32) trA<8 * r + 4 * hf>(lbase + ta);
        else {
          const v4u f = cat4(ldstr(L, ta), ldstr(L, ta + 512));
          if constexpr (inreg) {
            if constexpr (hf == 0) Wr[r - 32] = cat8(f, f); else Wr[r - 32] = cat8(__builtin_shufflevector(Wr[r - 32], Wr[r - 32], 0, 1, 2, 3), f);
          } else tmp[t * 2 + hf] = f;
        }
      });
      bar();
      if constexpr (ks + 6 <= 19) issue(ks + 6);
      if constexpr (kb >= 7 && hf == 1) {
#pragma unroll
        for (int t = (kb == 7 ? 2 : 0); t < 6; ++t) {
          const int i = kb == 7 ? t - 2 : (kb == 8 ? 4 + t : 10 + t);
          lds128w(L, s_wslot(i, w) + lane * 16, tmp[t * 2]);
          lds128w(L, s_wslot(i, w) + 1024 + lane * 16, tmp[t * 2 + 1]);
        }
      }
    });
  }
  const u32 pmask = page_mask(p);
  if (w == 0) s_loop<0>(p, L, h, pmask, Wr);
  else if (w == 1) s_loop<1>(p, L, h, pmask, Wr);
  else if (w == 2) s_loop<2>(p, L, h, pmask, Wr);
  else s_loop<3>(p, L, h, pmask, Wr);
}

constexpr u32 V_MS = 128 * 1024, V_VX = V_MS + 10240;
DEV u32 v_wslot(int i, int w) {
  return i < 6 ? 80 * 1024 + (w * 6 + i) * 2048 : (w * 10 + (i - 6)) * 2048;
}
template <int W> DEV void v_loop(const Params &p, LAS unsigned char *L, int h, u32 pmask, v8u (&Wo)[12]) {
  constexpr int w = W;
  const int tid = threadIdx.x, lane = tid & 63;
  const rsrc_t rh = mk_rsrc(p.ho, HO_BYTES), rw = mk_rsrc(p.ws, CTRL_BYTES);
  const int idx = (lane & 8) ? 0xEEEE : 0x4444;
  const int n16 = lane & 15, g = lane >> 4;
  const int q4 = (lane >> 2) & 3, pp = lane & 3;
  const u32 tra = opq(V_VX + (16 * g + (q4 & 1) + 4 * (q4 >> 1) + 2 * (pp >> 1)) * 16 + (pp & 1) * 8);
  const u32 wl0 = opq(v_wslot(0, w) + lane * 16), wl6 = opq(v_wslot(6, w) + lane * 16);
  const u32 vxw = opq(V_VX + (16 * 3 * w + n16) * 16 + (g & 1) * 8), ma = opq(V_MS + lane * 16);
  const u32 tin = h * 256 + n16 * 16 + (g & 1) * 8;
  bool ok = true;
  for (int s = 0; s < NSEG; ++s) {
    f32x4 ac1[3], acc[10];
#pragma unroll
    for (int t = 0; t < 3; ++t) { ac1[t] = f32x4{0.f, 0.f, 0.f, 0.f}; PIN_V(ac1[t]); }
#pragma unroll
    for (int t = 0; t < 10; ++t) { acc[t] = f32x4{0.f, 0.f, 0.f, 0.f}; PIN_V(acc[t]); }
    const u32 tbk = TBLK + 40 * s + 10 * w + (lane >> 5);
    u32 toff[5];
#pragma unroll
    for (int pr = 0; pr < 5; ++pr) { toff[pr] = blk_off(tbk + 2 * pr, pmask) + tin; PIN_V(toff[pr]); }
    v4u m[3];
    ok = get_M(rh, L, V_MS, s, pmask, tid, ok, W == 0, m);
    add1(rw, CB_T + (s - 1) * 128, lane == 0 && s > 0);
    v8u lb[4];
    auto lfrag = [&](auto U) {
      constexpr int u = U;
      constexpr int pr = u < 4 ? u / 2 : 2 + (u - 4) / 4, kb = u < 4 ? 2 : (((u - 4) % 4) < 2 ? 1 : 2), t = 2 * pr + (u & 1);
      constexpr int li = kb == 1 ? t - 4 : 6 + t;
      return lds_frag(L, li < 6 ? wl0 + li * 2048 : wl6 + (li - 6) * 2048);
    };
    {
      v4u A[10];
      A[own_kb(W, 0)] = m[0]; A[own_kb(W, 1)] = m[1]; A[own_kb(W, 2)] = m[2];
      float ev[4];
      sfor<0, 30>([&](auto O) {
        constexpr int o = O;
        constexpr int kb = o < 9 ? own_kb(W, o % 3) : (o < 23 ? rest_kb(W, (o - 9) / 2) : rest_kb(W, o - 23));
        constexpr int t = o < 9 ? o / 3 : (o < 23 ? (o - 9) & 1 : 2);
        if constexpr (o == 6) {
          bar();
          sfor<0, 7>([&](auto I) { A[rest_kb(W, I)] = lds128(L, ma + rest_kb(W, I) * 1024); });
        }
        smfA<8 * (kb * 3 + t)>(ac1[t], A[kb], idx);
        if constexpr (o == 27) asm volatile("s_nop 3" : "+v"(ac1[0]), "+v"(ac1[1]));
        if constexpr (o == 28) { ev[0] = swap32_sum(ac1[0][0], ac1[1][0]); ev[1] = swap32_sum(ac1[0][1], ac1[1][1]); }
        if constexpr (o == 29) { ev[2] = swap32_sum(ac1[0][2], ac1[1][2]); ev[3] = swap32_sum(ac1[0][3], ac1[1][3]); }
        if constexpr (o == 22) lb[0] = lfrag(std::integral_constant<int, 0>{});
        if constexpr (o == 23) lb[1] = lfrag(std::integral_constant<int, 1>{});
        if constexpr (o == 24) lb[2] = lfrag(std::integral_constant<int, 2>{});
        if constexpr (o == 25) lb[3] = lfrag(std::integral_constant<int, 3>{});
        SCHED();
      });
      lds64w(L, vxw + (lane >> 5) * 256, v2u{pk_bf16(ev[0], ev[1]), pk_bf16(ev[2], ev[3])});
      asm volatile("s_nop 4" : "+v"(ac1[2]));
      const float x = swap32_sum(ac1[2][0], ac1[2][2]), y = swap32_sum(ac1[2][1], ac1[2][3]);
      *(LAS u32 *)(L + vxw + 512 + (lane >> 5) * 4) = pk_bf16(x, y);
    }
    bar();
    {
      v4u A[3];
#pragma unroll
      for (int kb = 0; kb < 3; ++kb) A[kb] = cat4(ldstr(L, tra + kb * 1024), ldstr(L, tra + kb * 1024 + 128));
      SCHED();
      float ev[4];
      auto epi = [&](auto PR, auto STEP) {
        constexpr int pr = PR, st = STEP;
        if constexpr (st == 0) { ev[0] = swap32_sum(acc[2 * pr][0], acc[2 * pr + 1][0]); ev[1] = swap32_sum(acc[2 * pr][1], acc[2 * pr + 1][1]); }
        if constexpr (st == 1) { ev[2] = swap32_sum(acc[2 * pr][2], acc[2 * pr + 1][2]); ev[3] = swap32_sum(acc[2 * pr][3], acc[2 * pr + 1][3]); }
        if constexpr (st == 2) {
          auto sw = __builtin_amdgcn_permlane16_swap(pk_bf16(ev[0], ev[1]), pk_bf16(ev[2], ev[3]), false, false);
          st64c(rh, toff[pr], v2u{nosent(sw[0]), nosent(sw[1])});
        }
      };
      sfor<0, 30>([&](auto O) {
        constexpr int o = O, pr = o / 6, j = o % 6, kb = j / 2, t = 2 * pr + (j & 1);
        constexpr bool inreg = kb == 0 || (kb == 1 && t < 4);
        if constexpr (inreg) {
          constexpr int r = kb * 10 + t;
          if constexpr (r < 2) smfA<240 + 8 * r>(acc[t], A[kb], idx); else smfV(acc[t], A[kb], Wo[r - 2], idx);
        } else {
          constexpr int u = pr < 2 ? 2 * pr + (j & 1) : 4 + 4 * (pr - 2) + (kb == 1 ? 0 : 2) + (j & 1);
          smfV(acc[t], A[kb], lb[u % 4], idx);
          if constexpr (u + 4 < 16) lb[u % 4] = lfrag(std::integral_constant<int, u + 4>{});
        }
        if constexpr (o >= 14 && (o - 14) / 6 < 3 && (o - 14) % 6 < 3) epi(std::integral_constant<int, (o - 14) / 6>{}, std::integral_constant<int, (o - 14) % 6>{});
        SCHED();
      });
      asm volatile("s_nop 3" : "+v"(acc[6]), "+v"(acc[7]));
      sfor<0, 3>([&](auto ST) { epi(std::integral_constant<int, 3>{}, ST); });
      asm volatile("s_nop 0" : "+v"(acc[8]), "+v"(acc[9]));
      sfor<0, 3>([&](auto ST) { epi(std::integral_constant<int, 4>{}, ST); });
    }
    if (s + 2 < NSEG) {
#pragma unroll
      for (int pr = 0; pr < 5; ++pr) st64c(rh, blk_off(tbk + 80 + 2 * pr, pmask) + tin, v2u{SENT, SENT});
    }
  }
  WAIT_VM(0);
  add1(rw, CB_T + (NSEG - 1) * 128, lane == 0);
  st32c1(rw, CW_TMO * 4, 1u, !ok && lane == 0);
}

DEV void role_V(const Params &p, LAS unsigned char *L, int h) {
  const int tid = threadIdx.x, lane = tid & 63, w = __builtin_amdgcn_readfirstlane(tid >> 6);
  const u32 lbase = (u32)(uintptr_t)L;
  v8u Wo[12];
  {
    {
      const StageLane sl = stage_lane(lane, HD * 2);
      auto issue = [&](int ks) {
#pragma unroll
        for (int i = 0; i < 3; ++i) {
          const int n = w * 3 + i, ch = n >> 2, m = n & 3;
          const unsigned char *src = (const unsigned char *)(p.Wv + (size_t)ks * 32 * HD + h * DH + ch * 64);
          stage_instr(src, HD * 2, m, sl, lbase + (ks % 12) * 12288 + ch * 4096);
        }
      };
#pragma unroll
      for (int ks = 0; ks < 12; ++ks) issue(ks);
      sfor<0, 20>([&](auto KS) {
        constexpr int ks = KS;
        constexpr int later = (ks + 11 <= 19 ? 11 : 19 - ks);
        switch (later) {
          case 11: WAIT_VM(33); break; case 10: WAIT_VM(30); break; case 9: WAIT_VM(27); break; case 8: WAIT_VM(24); break;
          case 7: WAIT_VM(21); break; case 6: WAIT_VM(18); break; case 5: WAIT_VM(15); break; case 4: WAIT_VM(12); break;
          case 3: WAIT_VM(9); break; case 2: WAIT_VM(6); break; case 1: WAIT_VM(3); break; default: WAIT_VM(0); break;
        }
        bar();
        sfor<0, 3>([&](auto T) {
          constexpr int t = T, r = (ks >> 1) * 3 + t;
          trA<8 * r + 4 * (ks & 1)>(lbase + tr_addr_rt((ks % 12) * 12288, lane, 3 * w + t));
        });
        bar();
        if constexpr (ks + 12 <= 19) issue(ks + 12);
      });
    }
    {
      const StageLane sl = stage_lane(lane, D * 2);
      auto issue = [&](int ks) {
#pragma unroll
        for (int i = 0; i < 10; ++i) {
          const int n = w * 10 + i, ch = n >> 2, m = n & 3;
          const unsigned char *src = (const unsigned char *)(p.Wo + ((size_t)h * DH + ks * 32) * D + ch * 64);
          stage_instr(src, D * 2, m, sl, lbase + (ks % 4) * 40960 + ch * 4096);
        }
      };
#pragma unroll
      for (int ks = 0; ks < 4; ++ks) issue(ks);
      v4u tmp[20];
      sfor<0, 6>([&](auto KS) {
        constexpr int ks = KS, kb = ks >> 1, hf = ks & 1;
        if (ks <= 2) WAIT_VM(30); else if (ks == 3) WAIT_VM(20); else if (ks == 4) WAIT_VM(10); else WAIT_VM(0);
        bar();
        sfor<0, 10>([&](auto T) {
          constexpr int t = T, r = kb * 10 + t;
          const u32 ta = tr_addr_rt((ks % 4) * 40960, lane, 10 * w + t);
          constexpr bool inreg = kb == 0 || (kb == 1 && t < 4);
          if constexpr (inreg && r < 2) trA<240 + 8 * r + 4 * hf>(lbase + ta);
          else {
            const v4u f = cat4(ldstr(L, ta), ldstr(L, ta + 512));
            if constexpr (inreg) {
              if constexpr (hf == 0) Wo[r - 2] = cat8(f, f); else Wo[r - 2] = cat8(__builtin_shufflevector(Wo[r - 2], Wo[r - 2], 0, 1, 2, 3), f);
            } else tmp[t * 2 + hf] = f;
          }
        });
        bar();
        if constexpr (ks < 2) issue(ks + 4);
        if constexpr (kb >= 1 && hf == 1) {
#pragma unroll
          for (int t = (kb == 1 ? 4 : 0); t < 10; ++t) {
            const int i = kb == 1 ? t - 4 : 6 + t;
            lds128w(L, v_wslot(i, w) + lane * 16, tmp[t * 2]);
            lds128w(L, v_wslot(i, w) + 1024 + lane * 16, tmp[t * 2 + 1]);
          }
        }
      });
    }
  }
  const u32 pmask = page_mask(p);
  if (w == 0) v_loop<0>(p, L, h, pmask, Wo);
  else if (w == 1) v_loop<1>(p, L, h, pmask, Wo);
  else if (w == 2) v_loop<2>(p, L, h, pmask, Wo);
  else v_loop<3>(p, L, h, pmask, Wo);
}

DEV void role_O(const Params &p, LAS unsigned char *L, int c) {
  const u32 pmask = page_mask(p);
  const int tid = threadIdx.x, lane = tid & 63, w = __builtin_amdgcn_readfirstlane(tid >> 6);
  const rsrc_t rh = mk_rsrc(p.ho, HO_BYTES), ry = mk_rsrc(p.ws + OFF_YF, (u32)(OFF_KM - OFF_YF)), rw = mk_rsrc(p.ws, CTRL_BYTES);
  const int idx = (lane & 8) ? 0xEEEE : 0x4444;
  const int n16 = lane & 15, g = lane >> 4;
  const u32 rdw = ((n16 * 8 + 4 * (g & 1) + (lane >> 5)) * 4 + w) * 4;
  const u32 rdr = (((4 * (lane & 3) + 2 * (lane >> 5)) * 8 + ((lane >> 2) & 7)) * 4) * 4;
  const u32 min_ = (c >> 2) * 1024 + (c & 3) * 256 + lane * 4;
  bool ok = wait_ge(rw, CB_Y, 32);
  v4u Y[8];
#pragma unroll
  for (int i = 0; i < 8; ++i) Y[i] = ld128c(ry, (u32)(c * 32 + 8 * w + i) * 1024 + lane * 16);
  for (int s = 0; s < NSEG - 1; ++s) {
    v4u pv[7];
    f32x4 a0 = {0.f, 0.f, 0.f, 0.f};
    PIN_V(a0);
    u32 mdst = blk_off(MBLK + 2 * (s + 1) + (min_ >> 13), pmask) + (min_ & 8191);
    PIN_V(mdst);
    {
      const u32 pb = PBLK + 3 * s;
      const u32 ps = blk_off(pb + (w >> 1), pmask) + (w & 1) * 4096 + lane * 16;
      const u32 tt = blk_off(TBLK + 40 * s + c, pmask) + w * 2048 + lane * 16;
      const u32 off[7] = {ps, ps + 1024, ps + 2048, ps + 3072, blk_off(pb + 2, pmask) + w * 1024 + lane * 16, tt, tt + 1024};
      db_wait(rh, blk_off(DBLK, pmask), s + 1, w == 1 && ok);
      if (ok) ok = poll<7>(rh, off, pv);
    }
    smfV(a0, pv[0], cat8(Y[0], Y[1]), idx);
    smfV(a0, pv[1], cat8(Y[2], Y[3]), idx);
    smfV(a0, pv[2], cat8(Y[4], Y[5]), idx);
    smfV(a0, pv[3], cat8(Y[6], Y[7]), idx);
    smfV(a0, pv[4], cat8(pv[5], pv[6]), idx);
    MFMA_DONE(a0);
    const u32 dbuf = (s & 1) * 2048;
    {
      const float x = swap32_sum(a0[0], a0[1]), y = swap32_sum(a0[2], a0[3]);
      LAS float *d = (LAS float *)(L + dbuf + rdw);
      d[0] = x; d[8] = y;
    }
    bar();
    st32c1(rh, blk_off(DBLK, pmask) + 32 * 64, s + 1, tid == 3 * 64 && c == 0);
    if (w == 0) {
      const f32x4 x0 = *(LAS f32x4 *)(L + dbuf + rdr);
      const f32x4 x1 = *(LAS f32x4 *)(L + dbuf + rdr + 128);
      const u32 o = nosent(pk_bf16((x0[0] + x0[1]) + (x0[2] + x0[3]), (x1[0] + x1[1]) + (x1[2] + x1[3])));
      st32c(rh, mdst, o);
      if (s + 3 < NSEG) st32c(rh, blk_off(MBLK + 2 * (s + 3) + (min_ >> 13), pmask) + (min_ & 8191), SENT);
    }
    if (s + 2 < NSEG) {
      if (((s + 1) & 3) == 0) { if (ok) ok = wait_ge(rw, CB_Y + ((s + 1) >> 2) * 128, 32); }
#pragma unroll
      for (int i = 0; i < 8; ++i) Y[i] = ld128c(ry, (u32)(((s + 1) * 40 + c) * 32 + 8 * w + i) * 1024 + lane * 16);
    }
  }
  st32c1(rw, CW_TMO * 4, 1u, !ok && lane == 0);
}

constexpr u32 W_SLOT = 32768, W_EPI = 4 * W_SLOT, FAR = 0x40000000u;
DEV void dmab(rsrc_t r, u32 voff, u32 m0a, u32 m0b, u32 soa, u32 sob) {
  u32 tmp;
  asm volatile("s_add_u32 m0, %1, %2\n\ts_add_u32 %0, %3, %4\n\tbuffer_load_dwordx4 %5, %6, %0 offen lds"
               : "=&s"(tmp)
               : "s"(m0a), "s"(m0b), "s"(soa), "s"(sob), "v"(voff), "s"(r)
               : "m0", "scc", "memory");
}
DEV u32 rfl(u32 v) { return __builtin_amdgcn_readfirstlane(v); }

typedef float f32x16 __attribute__((ext_vector_type(16)));
DEV f32x16 mfma32(v4u a, v4u b, f32x16 c) {
  return __builtin_amdgcn_mfma_f32_32x32x16_bf16(__builtin_bit_cast(bf16x8, a), __builtin_bit_cast(bf16x8, b), c, 0, 0, 0);
}
DEV f32x16 zero16() { f32x16 z; for (int i = 0; i < 16; ++i) z[i] = 0.f; return z; }
template <int B> DEV v4u pack8(const f32x16 &a) {
  return v4u{pk_bf16(a[8 * B], a[8 * B + 1]), pk_bf16(a[8 * B + 2], a[8 * B + 3]), pk_bf16(a[8 * B + 4], a[8 * B + 5]), pk_bf16(a[8 * B + 6], a[8 * B + 7])};
}
DEV u32 wstage_lane(int lane, u32 rowstride_bytes) {
  const int r = lane >> 3, sg = (lane & 7) >> 1, hf = lane & 1;
  return r * rowstride_bytes + (16 * (sg ^ (r & 2)) + 8 * hf) * 2;
}
template <int ORD> DEV void wtr_bases(int lane, u32 (&tb)[2]) {
  const int gi = lane >> 4, q = (lane >> 2) & 3, p = lane & 3, h = gi >> 1, ch = gi & 1;
#pragma unroll
  for (int t = 0; t < 2; ++t) tb[t] = ((ORD == 0 ? 8 : 4) * h + q) * 128 + (((2 * t + ch) ^ (q & 2)) << 5) + 8 * p;
}
template <int ORD> DEV v4u wfrag(LAS unsigned char *L, u32 off, u32 tb) {
  return cat4(ldstr(L, off + tb), ldstr(L, off + tb + (ORD == 0 ? 512 : 1024)));
}

enum { T_QK = 0, T_VY = 1, T_C1 = 2, T_C2 = 3, T_PS = 4, T_FIN = 5, T_NONE = 6 };
struct Task { int type, a, b; };
struct WCtx {
  const Params &p;
  LAS unsigned char *L;
  u32 lbase;
  int w, lane, tid;
  rsrc_t ctl;
  u32 pend;
  bool ok;
  int q;
  u32 tkv;
  bool asked;
};
DEV void flush_pend(WCtx &c) { add1(c.ctl, c.pend, threadIdx.x == 0); c.pend = OOB; }
DEV void drain_pend(WCtx &c) { WAIT_VM(0); bar(); flush_pend(c); }
DEV void wait_wg(WCtx &c, u32 cb, u32 want) {
  if (c.w == 0 && c.ok) c.ok = wait_ge(c.ctl, cb, want);
  bar();
}

template <int N> DEV void wait_vm() { asm volatile("s_waitcnt vmcnt(%0)" ::"n"(N) : "memory"); }
template <int NT> struct XW {
  static constexpr bool DOWN = NT == 10;
  static constexpr int NCH = NT / 2, NI = NCH + 2, NST = DOWN ? 48 : 20;
  rsrc_t rw, rx;
  u32 v, lx, c[NCH], l[NCH], step, xso, xl, lbase;
  DEV XW(const WCtx &cx, const u16 *W0, const u16 *W1, u32 rs, int seg) {
    const int w = cx.w, lane = cx.lane;
    const u16 *Wm = (NT == 12 && w >= 2) ? W1 : W0;
    rw = mk_rsrc(Wm, NST * 32 * rs);
    rx = DOWN ? mk_rsrc(cx.p.ws + OFF_HF, (u32)(OFF_GT - OFF_HF)) : mk_rsrc(cx.p.ws + OFF_XN, 2048 * 640 * 2);
    v = wstage_lane(lane, rs);
    lx = lane * 16;
#pragma unroll
    for (int i = 0; i < NCH; ++i) {
      const int n = w * NCH + i, ch = n >> 2, m = n & 3;
      c[i] = rfl((NT == 12 ? ch % 3 : ch) * 128 + m * 8 * rs);
      l[i] = rfl(ch * 4096 + m * 1024);
    }
    step = 32 * rs; xso = seg * (NST * 2048); xl = 24576 + w * 2048; lbase = cx.lbase;
  }
  struct St { u32 slot, sw, sx; };
  DEV St where(int ks) const {
    return St{rfl(lbase + (ks & 3) * W_SLOT), rfl(ks < NST ? ks * step : FAR), rfl(ks < NST ? xso + ks * 2048 : FAR)};
  }
  template <int I> DEV void issue1(const St &t) const {
    if constexpr (I < NCH) dmab(rw, v, t.slot, l[I], t.sw, c[I]);
    else dmab(rx, lx, t.slot, xl + (I - NCH) * 1024, t.sx, (I - NCH) * 1024);
  }
  DEV void issue(int ks) const { const St t = where(ks); sfor<0, NI>([&](auto I) { issue1<I>(t); }); }
  DEV void prologue() const { issue(0); issue(1); issue(2); }
};
template <int NT> DEV void xw_main(WCtx &cx, const XW<NT> &x, f32x16 (&acc)[NT]) {
  constexpr bool DOWN = XW<NT>::DOWN;
  constexpr int NI = XW<NT>::NI, NST = XW<NT>::NST, NO_ = 2 * NT, ORD = DOWN ? 1 : 0;
  LAS unsigned char *L = cx.L;
  u32 tb[2];
  wtr_bases<ORD>(cx.lane, tb);
  wait_vm<2 * NI>();
  bar();
  const u32 xo = 24576 + cx.w * 2048 + cx.lane * 16;
  auto frag = [&](u32 so, auto O) { constexpr int o = O, T = o % NT, k = o / NT; return wfrag<ORD>(L, so + (T >> 1) * 4096 + k * 2048, tb[T & 1]); };
  constexpr int RING = NO_ % 6 == 0 ? 6 : 5, DIST = 4;
  static_assert(NO_ % RING == 0 && DIST < RING && NI <= NO_ / 2 && NO_ - DIST > NO_ / 2, "fragment ring / DMA slots");
  v4u B[2], A[RING];
  B[0] = lds128(L, xo); B[1] = lds128(L, xo + 1024);
  sfor<0, DIST>([&](auto O) { A[O] = frag(0, O); });
  auto mm = [&](const v4u &wf, const v4u &xf, const f32x16 &c) { return DOWN ? mfma32(xf, wf, c) : mfma32(wf, xf, c); };
  auto stage = [&](auto FIRST, int ks) {
    const u32 so = (ks & 3) * W_SLOT, sn = ((ks + 1) & 3) * W_SLOT;
    v4u N0;
    const typename XW<NT>::St nxt = x.where(ks + 3);
    sfor<0, NO_>([&](auto OO) {
      constexpr int o = OO;
      if constexpr (o == NO_ / 2) {
        wait_vm<NI>();
        bar_raw();
        if (ks == 1) flush_pend(cx);
      }
      if constexpr (o + DIST < NO_) A[(o + DIST) % RING] = frag(so, std::integral_constant<int, o + DIST>{});
      else A[(o + DIST) % RING] = frag(sn, std::integral_constant<int, o + DIST - NO_>{});
      if constexpr (o == NO_ - 3) N0 = lds128(L, sn + xo);
      SCHED();
      if constexpr (FIRST && o < NT) acc[o] = mm(A[o % RING], B[0], zero16());
      else acc[o % NT] = mm(A[o % RING], B[o / NT], acc[o % NT]);
      if constexpr (o >= NO_ / 2 && o - NO_ / 2 < NI) x.template issue1<o - NO_ / 2>(nxt);
      SCHED();
    });
    B[0] = N0;
    B[1] = lds128(L, sn + xo + 1024);
  };
  stage(std::true_type{}, 0);
  for (int ks = 1; ks < NST; ++ks) stage(std::false_type{}, ks);
  WAIT_VM(0);
  bar();
}

struct YW {
  rsrc_t rw;
  u32 v, c[6], l[6], lbase;
  DEV YW(const WCtx &cx, int h) {
    rw = mk_rsrc(cx.p.Wo + (size_t)h * DH * D, DH * D * 2);
    v = wstage_lane(cx.lane, D * 2);
#pragma unroll
    for (int i = 0; i < 6; ++i) {
      const int n = cx.w * 6 + i, ch = n >> 2, m = n & 3;
      c[i] = rfl(ch * 32 * (D * 2) + m * 8 * (D * 2));
      l[i] = rfl(ch * 4096 + m * 1024);
    }
    lbase = cx.lbase;
  }
  template <int I> DEV void issue1(u32 so, int sl) const { dmab(rw, v, rfl(lbase + (sl & 3) * W_SLOT), l[I], so, c[I]); }
  DEV void issue(int cg) const { sfor<0, 6>([&](auto I) { issue1<I>(rfl(cg * 128), cg); }); }
};

constexpr int SBIG = 58;
constexpr int NQ_PROJ = 256, NQ_GLU = 16 * 10, NQ_FIN = 2 + (SBIG - 1) * 4 + 10 + (63 - SBIG) * 18 + 10;
DEV Task ps_task(int s, int i) { return s < SBIG ? Task{T_PS, s, i | 256} : Task{T_PS, s, i}; }
DEV Task fin_task(int s, int i) { return s < SBIG ? Task{T_FIN, s, (10 * i) | (10 << 8)} : Task{T_FIN, s, (2 * i) | (2 << 8)}; }
DEV Task decode_task(int q, int t) {
  Task k;
  if (q < 4) {
    int tb, combo;
    if (t < 64) { tb = (t >> 5) * 2 + (t & 1); combo = (t & 31) >> 1; }
    else { const int u = t - 64; tb = 4 + 4 * (u >> 6) + (u & 3); combo = (u & 63) >> 2; }
    k.type = (combo & 1) ? T_QK : T_VY; k.a = tb; k.b = 8 * q + (combo >> 1);
  } else if (q == 4) {
    if (t < 8) { k.type = T_C1; k.a = 0; k.b = t; }
    else {
      const int u = t - 8, blk = u / 10, r = u % 10;
      if (blk < 15 && r < 8) { k.type = T_C1; k.a = blk + 1; k.b = r; }
      else { k.type = T_C2; k.a = blk; k.b = blk < 15 ? r - 8 : r; }
    }
  } else {
    constexpr int E1 = 2 + (SBIG - 1) * 4, E2 = E1 + 10, E3 = E2 + (63 - SBIG) * 18;
    if (t < 2) k = ps_task(0, t);
    else if (t < E1) { const int u = t - 2, sg = 1 + (u >> 2), r = u & 3; k = r < 2 ? ps_task(sg, r) : fin_task(sg - 1, r - 2); }
    else if (t < E2) { const int r = t - E1; k = r < 8 ? ps_task(SBIG, r) : fin_task(SBIG - 1, r - 8); }
    else if (t < E3) { const int u = t - E2, sg = SBIG + 1 + u / 18, r = u % 18; k = r < 8 ? ps_task(sg, r) : fin_task(sg - 1, r - 8); }
    else k = fin_task(63, t - E3);
  }
  return k;
}
DEV int share_ticket(WCtx &c, u32 v) {
  LAS u32 *tk = (LAS u32 *)(c.L + W_EPI + 32768 - 16);
  tk[c.tid == 0 ? 0 : 1] = v;
  bar();
  const int t = __builtin_amdgcn_readfirstlane(tk[0]);
  bar();
  return t;
}
DEV void ask(WCtx &c) { c.tkv = add1r(c.ctl, CB_Q + c.q * 128, c.tid == 0); c.asked = true; }
DEV Task take(WCtx &c) {
  if (!c.asked) ask(c);
  c.asked = false;
  int t = share_ticket(c, c.tkv);
  for (;;) {
    const int n = c.q < 4 ? NQ_PROJ : (c.q == 4 ? NQ_GLU : NQ_FIN);
    if (t < n) return decode_task(c.q, t);
    if (c.q == 5) return Task{T_NONE, 0, 0};
    c.q = c.q < 4 ? 4 : 5;
    t = share_ticket(c, add1r(c.ctl, CB_Q + c.q * 128, c.tid == 0));
  }
}

DEV XW<12> make_qk(const WCtx &c, const Task &t) { return XW<12>(c, c.p.Wq + t.b * DH, c.p.Wk + t.b * DH, HD * 2, 4 * t.a + c.w); }
DEV XW<12> make_c1(const WCtx &c, const Task &t) { return XW<12>(c, c.p.Wg + t.b * 192, c.p.Wu + t.b * 192, FF * 2, 4 * t.a + c.w); }
DEV XW<6> make_v(const WCtx &c, const Task &t) { return XW<6>(c, c.p.Wv + t.b * DH, c.p.Wv, HD * 2, 4 * t.a + c.w); }
DEV bool pre_issue(const WCtx &c, const Task &t) {
  if (t.type == T_QK) { make_qk(c, t).prologue(); return true; }
  if (t.type == T_C1) { make_c1(c, t).prologue(); return true; }
  if (t.type == T_VY) { make_v(c, t).prologue(); return true; }
  return false;
}

DEV bool task_QK(WCtx &c, const Task &t, bool pre, Task &nx) {
  const Params &p = c.p;
  const int tb = t.a, h = t.b, s = 4 * tb + c.w, lane = c.lane;
  f32x16 acc[12];
  {
    const XW<12> x = make_qk(c, t);
    if (!pre) x.prologue();
    xw_main<12>(c, x, acc);
  }
  nx = take(c);
  bool npre = tb >= 2 && pre_issue(c, nx);
  const rsrc_t rq = mk_rsrc(p.ws + OFF_KF, (u32)(OFF_YF - OFF_KF));
  const u32 fo = (u32)(s * 32 + h) * 12288 + ((lane >> 4) & 1) * 6144 + ((lane & 15) + 16 * (lane >> 5)) * 16;
  f32x16 sx = zero16();
  sfor<0, 6>([&](auto TT) {
    constexpr int T = TT;
    const v4u q0 = pack8<0>(acc[T]), q1 = pack8<1>(acc[T]), k0 = pack8<0>(acc[6 + T]), k1 = pack8<1>(acc[6 + T]);
    st128c(rq, fo + T * 1024, k0);
    st128c(rq, fo + T * 1024 + 512, k1);
    st128c(rq, (u32)(OFF_QF - OFF_KF) + fo + T * 1024, q0);
    st128c(rq, (u32)(OFF_QF - OFF_KF) + fo + T * 1024 + 512, q1);
    sx = mfma32(k0, q0, sx);
    sx = mfma32(k1, q1, sx);
  });
#pragma unroll
  for (int i = 0; i < 4; ++i)
    st128c(rq, (u32)(OFF_SX - OFF_KF) + (u32)(s * 32 + h) * 4096 + i * 1024 + lane * 16,
           __builtin_bit_cast(v4u, f32x4{sx[4 * i], sx[4 * i + 1], sx[4 * i + 2], sx[4 * i + 3]}));
  c.pend = CB_KF + (tb * 32 + h) * 128;
  if (tb < 2) { drain_pend(c); npre = pre_issue(c, nx); }
  return npre;
}

DEV bool task_C1(WCtx &c, const Task &t, bool pre, Task &nx) {
  const Params &p = c.p;
  const int tb = t.a, fb = t.b, s = 4 * tb + c.w, lane = c.lane;
  f32x16 acc[12];
  {
    const XW<12> x = make_c1(c, t);
    if (!pre) x.prologue();
    xw_main<12>(c, x, acc);
  }
  nx = take(c);
  const bool npre = pre_issue(c, nx);
  const rsrc_t rf = mk_rsrc(p.ws + OFF_HF, (u32)(OFF_GT - OFF_HF));
  sfor<0, 6>([&](auto TT) {
    constexpr int T = TT;
    f32x16 hv;
#pragma unroll
    for (int r = 0; r < 16; ++r) hv[r] = rbf(acc[T][r]) * rbf(acc[6 + T][r]);
    const u32 o = (u32)(s * 96 + 12 * fb + 2 * T) * 1024 + lane * 16;
    st128c(rf, o, pack8<0>(hv));
    st128c(rf, o + 1024, pack8<1>(hv));
  });
  c.pend = CB_H + tb * 128;
  return npre;
}

DEV bool task_VY(WCtx &c, const Task &t, bool pre, Task &nx) {
  const Params &p = c.p;
  LAS unsigned char *L = c.L;
  const int tb = t.a, h = t.b, s = 4 * tb + c.w, lane = c.lane;
  v4u Vf[12];
  const YW yw(c, h);
  {
    f32x16 acc[6];
    {
      const XW<6> x = make_v(c, t);
      if (!pre) x.prologue();
      xw_main<6>(c, x, acc);
    }
    yw.issue(0); yw.issue(1); yw.issue(2);
#pragma unroll
    for (int T = 0; T < 6; ++T) { Vf[2 * T] = pack8<0>(acc[T]); Vf[2 * T + 1] = pack8<1>(acc[T]); }
  }
  u32 tb2[2];
  wtr_bases<1>(lane, tb2);
  const rsrc_t ry = mk_rsrc(p.ws + OFF_YF, (u32)(OFF_KM - OFF_YF));
  auto frag = [&](u32 so, auto O) { constexpr int o = O, tp = o / 12, k = o % 12; return wfrag<1>(L, so + (k >> 1) * 4096 + (k & 1) * 2048, tb2[tp]); };
  WAIT_VM(12);
  bar();
  constexpr int RING = 6, DIST = 4;
  v4u B[RING];
  sfor<0, DIST>([&](auto O) { B[O] = frag(0, O); });
  f32x16 y[2] = {zero16(), zero16()};
  const u32 yb = (((u32)(s * 40) + ((lane >> 4) & 1)) * 32 + h) * 1024 + ((lane & 15) + 16 * (lane >> 5)) * 16;
  u32 yprev = OOB;
  for (int cg = 0; cg < 10; ++cg) {
    const u32 so = (cg & 3) * W_SLOT, sn = ((cg + 1) & 3) * W_SLOT;
    const u32 yo = yb + cg * (4 * 32768), ynx = rfl(cg + 3 < 10 ? (cg + 3) * 128 : FAR);
    sfor<0, 24>([&](auto OO) {
      constexpr int o = OO, tp = o / 12, k = o % 12;
      if constexpr (o == 12) {
        WAIT_VM(8);
        bar_raw();
      }
      if constexpr (o + DIST < 24) B[(o + DIST) % RING] = frag(so, std::integral_constant<int, o + DIST>{});
      else B[(o + DIST) % RING] = frag(sn, std::integral_constant<int, o + DIST - 24>{});
      SCHED();
      if constexpr (k == 0) y[tp] = mfma32(Vf[0], B[o % RING], zero16());
      else y[tp] = mfma32(Vf[k], B[o % RING], y[tp]);
      if constexpr (o >= 12 && o < 18) yw.template issue1<o - 12>(ynx, cg + 3);
      if constexpr (k == 3) {
        const u32 d = tp > 0 ? yo : yprev;
        st128c(ry, d, pack8<0>(y[tp ^ 1]));
        st128c(ry, d + 512, pack8<1>(y[tp ^ 1]));
      }
      SCHED();
    });
    yprev = yo + 2 * 32768;
  }
  WAIT_VM(0);
  bar();
  nx = take(c);
  bool npre = tb >= 2 && pre_issue(c, nx);
  st128c(ry, yprev, pack8<0>(y[1]));
  st128c(ry, yprev + 512, pack8<1>(y[1]));
  c.pend = CB_Y + tb * 128;
  if (tb < 2) { drain_pend(c); npre = pre_issue(c, nx); }
  return npre;
}

DEV void task_C2(WCtx &c, const Task &t, Task &nx) {
  const Params &p = c.p;
  LAS unsigned char *L = c.L;
  const int tb = t.a, cb = t.b, w = c.w, lane = c.lane, s = 4 * tb + w;
  drain_pend(c);
  wait_wg(c, CB_H + tb * 128, 8);
  __builtin_amdgcn_fence(__ATOMIC_ACQUIRE, "agent");
  WAIT_VM(0);
  bar();
  f32x16 acc[10];
  {
    const XW<10> x(c, p.Wd + cb * 320, p.Wd, D * 2, s);
    x.prologue();
    xw_main<10>(c, x, acc);
  }
  const u32 eb = opq(w * 32 * 656 + 4 * (lane >> 5) * 656 + (lane & 31) * 2);
#pragma unroll
  for (int T = 0; T < 10; ++T)
#pragma unroll
    for (int r = 0; r < 16; ++r) {
      const float d = rbf(acc[T][r]);
      const float gt = d * __builtin_amdgcn_rcpf(1.0f + __builtin_amdgcn_exp2f(d * -1.4426950408889634f));
      *(LAS u16 *)(L + eb + ((r & 3) + 8 * (r >> 2)) * 656 + 64 * T) = (u16)(pk_bf16(gt, 0.f) & 0xFFFFu);
    }
  WAIT_LGKM0();
  const rsrc_t rg = mk_rsrc(p.ws + OFF_GT, 2048 * 640 * 2);
#pragma unroll
  for (int i = 0; i < 20; ++i) {
    const int it = i * 64 + lane, row = it / 40, pc = it % 40;
    st128c(rg, (u32)(s * 32 + row) * 1280 + cb * 640 + pc * 16, lds128(L, w * 32 * 656 + row * 656 + pc * 16));
  }
  c.pend = CB_G + (tb * 5 + cb) * 128;
  nx = take(c);
}

DEV void settle(WCtx &c) { WAIT_VM(0); bar_raw(); flush_pend(c); }

struct PsIn { v4u Qf[12], km[12], sxr[4]; };
DEV void ps_load_q(rsrc_t rw, int s, int h, int q32, int hf, int lane, PsIn &in) {
  const u32 qo = (u32)OFF_QF + (u32)(s * 32 + h) * 12288 + (q32 >> 4) * 6144 + ((q32 & 15) + 16 * hf) * 16;
#pragma unroll
  for (int k = 0; k < 12; ++k) in.Qf[k] = ld128c(rw, qo + (k >> 1) * 1024 + (k & 1) * 512);
#pragma unroll
  for (int i = 0; i < 4; ++i) in.sxr[i] = ld128c(rw, (u32)OFF_SX + (u32)(s * 32 + h) * 4096 + i * 1024 + lane * 16);
}
DEV void ps_load_k(rsrc_t rw, int s, int h, int q32, int hf, PsIn &in) {
  const u32 ko = (u32)OFF_KM + (u32)(s * 32 + h) * 3072 + hf * 128 + (q32 & 7) * 16;
#pragma unroll
  for (int k = 0; k < 12; ++k) in.km[k] = ld128c(rw, ko + (k >> 1) * 512 + (k & 1) * 256);
}
DEV void ps_load(rsrc_t rw, int s, int h, int q32, int hf, int lane, PsIn &in) {
  ps_load_k(rw, s, h, q32, hf, in);
  ps_load_q(rw, s, h, q32, hf, lane, in);
}
DEV void ps_head(rsrc_t rw, int s, int h, int q32, int hf, const PsIn &in) {
  const float sc = 0.07216878364870322f * 1.4426950408889634f, NI = -INFINITY;
  f32x16 sm = zero16();
#pragma unroll
  for (int k = 0; k < 12; ++k) sm = mfma32(in.km[k], in.Qf[k], sm);
  f32x16 x;
  float mx = vmax(vmax3(sm[0], sm[1], sm[2]), sm[3]);
#pragma unroll
  for (int r = 0; r < 16; ++r) {
    const int key = (r & 3) + 8 * (r >> 2) + 4 * hf;
    x[r] = key <= q32 ? u2f(in.sxr[r >> 2][r & 3]) : NI;
    mx = vmax(mx, x[r]);
  }
  mx = rmax32(mx);
  float sum = 0.f;
#pragma unroll
  for (int r = 0; r < 4; ++r) { sm[r] = __builtin_amdgcn_exp2f((sm[r] - mx) * sc); sum += sm[r]; }
#pragma unroll
  for (int r = 0; r < 16; ++r) { x[r] = __builtin_amdgcn_exp2f((x[r] - mx) * sc); sum += x[r]; }
  const float inv = 1.0f / xsum32(sum);
#pragma unroll
  for (int r = 0; r < 16; ++r) x[r] *= inv;
  const u32 pb = (u32)OFF_PS + (u32)(s * 2 + (q32 >> 4)) * 40 * 1024;
  st128c(rw, pb + h * 1024 + ((q32 & 15) + 16 * hf) * 16, pack8<0>(x));
  st128c(rw, pb + h * 1024 + ((q32 & 15) + 16 * (hf + 2)) * 16, pack8<1>(x));
  auto sw = __builtin_amdgcn_permlane32_swap(pk_bf16(sm[0] * inv, sm[1] * inv), pk_bf16(sm[2] * inv, sm[3] * inv), false, false);
  st64c(rw, pb + (32 + (h >> 2)) * 1024 + (16 * (h & 3) + (q32 & 15)) * 16 + hf * 8, v2u{sw[0], sw[1]});
}
DEV void task_PS(WCtx &c, const Task &t, Task &nx) {
  const Params &p = c.p;
  const int s = t.a, w = c.w, lane = c.lane, q32 = lane & 31, hf = lane >> 5;
  const bool big = t.b >> 8;
  const int h0 = big ? 16 * (t.b & 255) + 4 * w : 4 * t.b + w, nh = big ? 4 : 1;
  settle(c);
  for (int i = 0; i < nh; ++i) { if (c.ok) c.ok = wait_ge(c.ctl, CB_KF + ((s >> 2) * 32 + h0 + i) * 128, 1); }
  __builtin_amdgcn_fence(__ATOMIC_ACQUIRE, "wavefront");
  const rsrc_t rw = mk_rsrc(p.ws, (u32)OFF_HO);
  PsIn i0, i1;
  ps_load_q(rw, s, h0, q32, hf, lane, i0);
  if (c.ok) c.ok = wait_ge(c.ctl, CB_KM + s * 128, 32);
  __builtin_amdgcn_fence(__ATOMIC_ACQUIRE, "wavefront");
  ps_load_k(rw, s, h0, q32, hf, i0);
  if (big) {
    ps_load(rw, s, h0 + 1, q32, hf, lane, i1);
    ps_head(rw, s, h0, q32, hf, i0);
    ps_load(rw, s, h0 + 2, q32, hf, lane, i0);
    ps_head(rw, s, h0 + 1, q32, hf, i1);
    ps_load(rw, s, h0 + 3, q32, hf, lane, i1);
    ps_head(rw, s, h0 + 2, q32, hf, i0);
    ps_head(rw, s, h0 + 3, q32, hf, i1);
  } else ps_head(rw, s, h0, q32, hf, i0);
  c.pend = CB_P + s * 128;
  nx = take(c);
  if (!big) drain_pend(c);
}

struct FinIn { v4u B[10][2]; v2u xg, xx; };
DEV void task_FIN(WCtx &c, const Task &t, Task &nx) {
  const Params &p = c.p;
  const u32 pmask = page_mask(p);
  LAS unsigned char *L = c.L;
  const int s = t.a, g0 = t.b & 255, ng = t.b >> 8, w = c.w, lane = c.lane, n16 = lane & 15, g = lane >> 4, tid = c.tid;
  settle(c);
  if (w == 0 && c.ok) {
    c.ok = wait_ge(c.ctl, CB_Y + (s >> 2) * 128, 32);
    if (c.ok) c.ok = wait_ge(c.ctl, CB_G + ((s >> 2) * 5 + g0 / 10) * 128, 1);
    if (c.ok) c.ok = wait_ge(c.ctl, CB_T + s * 128, 128);
  }
  bar();
  __builtin_amdgcn_fence(__ATOMIC_ACQUIRE, "wavefront");
  const rsrc_t rw = mk_rsrc(p.ws, (u32)OFF_HO), rh = mk_rsrc(p.ho, HO_BYTES), rx = mk_rsrc(p.x, 2048 * 640 * 2), ro = mk_rsrc(p.out, 2048 * 640 * 2);
  v4u A[10][2];
  const u32 eo = (u32)(s * 32 + (tid >> 3)) * 1280 + (tid & 7) * 8;
  auto load = [&](int gr, FinIn &in) {
#pragma unroll
    for (int ct = 0; ct < 2; ++ct) {
      const u32 yb = (u32)OFF_YF + (u32)((s * 40 + 2 * gr + ct) * 32 + 8 * w) * 1024 + lane * 16;
      const u32 tb = blk_off(TBLK + 40 * s + 2 * gr + ct, pmask) + 2 * w * 1024 + lane * 16;
#pragma unroll
      for (int k = 0; k < 8; ++k) in.B[k][ct] = ld128c(rw, yb + k * 1024);
      in.B[8][ct] = ld128c(rh, tb);
      in.B[9][ct] = ld128c(rh, tb + 1024);
    }
    in.xg = __builtin_amdgcn_raw_buffer_load_b64(rw, (int)((u32)OFF_GT + eo + gr * 64), 0, 16);
    in.xx = __builtin_amdgcn_raw_buffer_load_b64(rx, (int)(eo + gr * 64), 0, 0);
  };
  auto group = [&](int gr, const FinIn &in, u32 buf) {
    f32x4 acc[2][2];
#pragma unroll
    for (int ct = 0; ct < 2; ++ct) { acc[ct][0] = f32x4{0.f, 0.f, 0.f, 0.f}; acc[ct][1] = f32x4{0.f, 0.f, 0.f, 0.f}; }
#pragma unroll
    for (int k = 0; k < 10; ++k)
#pragma unroll
      for (int ct = 0; ct < 2; ++ct) {
        acc[ct][0] = mfma(A[k][0], in.B[k][ct], acc[ct][0]);
        acc[ct][1] = mfma(A[k][1], in.B[k][ct], acc[ct][1]);
      }
#pragma unroll
    for (int ct = 0; ct < 2; ++ct)
#pragma unroll
      for (int tq = 0; tq < 2; ++tq)
#pragma unroll
        for (int r = 0; r < 4; ++r) *(LAS float *)(L + buf + w * 4608 + (16 * tq + 4 * g + r) * 144 + (16 * ct + n16) * 4) = acc[ct][tq][r];
    bar();
    const u32 lo = buf + (tid >> 3) * 144 + (tid & 7) * 16;
    f32x4 v = *(LAS f32x4 *)(L + lo);
#pragma unroll
    for (int ww = 1; ww < 4; ++ww) v += *(LAS f32x4 *)(L + ww * 4608 + lo);
    const float g0_ = bf2f(in.xg.x & 0xFFFFu), g1 = bf2f(in.xg.x >> 16), g2 = bf2f(in.xg.y & 0xFFFFu), g3 = bf2f(in.xg.y >> 16);
    const float x0 = bf2f(in.xx.x & 0xFFFFu), x1 = bf2f(in.xx.x >> 16), x2 = bf2f(in.xx.y & 0xFFFFu), x3 = bf2f(in.xx.y >> 16);
    const v2u o = {pk_bf16(x0 + rbf(rbf(v[0]) * g0_), x1 + rbf(rbf(v[1]) * g1)), pk_bf16(x2 + rbf(rbf(v[2]) * g2), x3 + rbf(rbf(v[3]) * g3))};
    __builtin_amdgcn_raw_buffer_store_b64(o, ro, (int)(eo + gr * 64), 0, 0);
  };
  FinIn i0, i1;
  load(g0, i0);
  load(g0 + 1, i1);
  if (w == 0 && c.ok) c.ok = wait_ge(c.ctl, CB_P + s * 128, s < SBIG ? 2 : 8);
  bar();
  __builtin_amdgcn_fence(__ATOMIC_ACQUIRE, "wavefront");
#pragma unroll
  for (int k = 0; k < 10; ++k) {
    const int ks = k < 8 ? 8 * w + k : 32 + 2 * w + (k - 8);
#pragma unroll
    for (int tq = 0; tq < 2; ++tq) A[k][tq] = ld128c(rw, (u32)OFF_PS + (u32)((s * 2 + tq) * 40 + ks) * 1024 + lane * 16);
  }
  for (int gr = g0; gr < g0 + ng; gr += 2) {
    group(gr, i0, 0);
    if (gr + 2 < g0 + ng) load(gr + 2, i0);
    group(gr + 1, i1, 32768);
    if (gr + 3 < g0 + ng) load(gr + 3, i1);
  }
  nx = take(c);
}

DEV void role_W(const Params &p, LAS unsigned char *L, int q0, int rank) {
  const int tid = threadIdx.x;
  WCtx c{p, L, (u32)(uintptr_t)L, (int)__builtin_amdgcn_readfirstlane(tid >> 6), tid & 63, tid, mk_rsrc(p.ws, CTRL_BYTES), OOB, true, q0, 0u, false};
  Task cur = decode_task(q0, rank);
  bool pre = false;
  while (cur.type != T_NONE) {
    asm volatile("" : "+v"(c.lane), "+v"(c.tid));
    if (cur.type < T_C2) ask(c);
    Task nx;
    if (cur.type == T_QK) pre = task_QK(c, cur, pre, nx);
    else if (cur.type == T_VY) pre = task_VY(c, cur, pre, nx);
    else if (cur.type == T_C1) pre = task_C1(c, cur, pre, nx);
    else {
      if (cur.type == T_C2) task_C2(c, cur, nx);
      else if (cur.type == T_PS) task_PS(c, cur, nx);
      else task_FIN(c, cur, nx);
      pre = false;
    }
    cur = nx;
  }
  drain_pend(c);
  st32c1(c.ctl, CW_TMO * 4, 1u, !c.ok && tid == 0);
}

__global__ void __launch_bounds__(256) k_pre(Params p) {
  const int tid = threadIdx.x, lane = tid & 63, w = tid >> 6, b = blockIdx.x;
#pragma unroll
  for (int rr = 0; rr < 2; ++rr) {
    const int row = (b * 4 + w) * 2 + rr;
    v4u a = {0, 0, 0, 0}, c = {0, 0, 0, 0};
    if (lane < 40) {
      const v4u *src = (const v4u *)(p.x + (size_t)row * D + lane * 16);
      a = src[0]; c = src[1];
    }
    float f[16];
#pragma unroll
    for (int i = 0; i < 4; ++i) { f[2 * i] = bf2f(a[i] & 0xFFFF); f[2 * i + 1] = bf2f(a[i] >> 16); f[8 + 2 * i] = bf2f(c[i] & 0xFFFF); f[8 + 2 * i + 1] = bf2f(c[i] >> 16); }
    float sum = 0.f;
#pragma unroll
    for (int i = 0; i < 16; ++i) sum += f[i];
#pragma unroll
    for (int o = 32; o > 0; o >>= 1) sum += __shfl_xor(sum, o);
    const float mean = sum * (1.0f / D);
    float vs = 0.f;
    if (lane < 40) {
#pragma unroll
      for (int i = 0; i < 16; ++i) vs += (f[i] - mean) * (f[i] - mean);
    }
#pragma unroll
    for (int o = 32; o > 0; o >>= 1) vs += __shfl_xor(vs, o);
    const float mb = rbf(mean), vb = rbf(vs * (1.0f / D));
    const float den = rbf(sqrtf(rbf(vb + 1e-5f)));
    if (lane < 40) {
      u32 o[8];
#pragma unroll
      for (int i = 0; i < 8; ++i) o[i] = pk_bf16(rbf(f[2 * i] - mb) / den, rbf(f[2 * i + 1] - mb) / den);
      const int seg = row >> 5, tok = row & 31;
      unsigned char *dst = p.ws + OFF_XN + ((size_t)seg * 40 + lane) * 1024 + tok * 16;
      *(v4u *)dst = v4u{o[0], o[1], o[2], o[3]};
      *(v4u *)(dst + 512) = v4u{o[4], o[5], o[6], o[7]};
    }
  }
  const int gt = b * 256 + tid;
  if (gt < (int)(CTRL_BYTES / 16)) {
    const int qh = (gt * 16 - (int)CB_Q) / 128;
    const bool head = gt * 16 >= (int)CB_Q && (gt * 16 - (int)CB_Q) % 128 == 0 && qh < 5;
    ((v4u *)p.ws)[gt] = v4u{head ? (qh < 4 ? 32u : 256u - NCHAIN - 128u) : 0u, 0, 0, 0};
  }
  {
    const v4u sv = {SENT, SENT, SENT, SENT};
    for (int i = gt; i < (4 + 6 + 80) * 1024; i += 65536) {
      const int bl = i >> 10, blk = bl < 4 ? MBLK + 2 + bl : (bl < 10 ? PBLK + (bl - 4) : TBLK + (bl - 10));
      *(v4u *)(p.ho + ((size_t)blk << 14) + (i & 1023) * 16) = sv;
    }
  }
  if (gt >= 61440 && gt < 61440 + 1024) *(v4u *)(p.ho + ((size_t)DBLK << 14) + (gt - 61440) * 16) = v4u{0, 0, 0, 0};
  if (gt < 2 * 2560) {
    const int par = gt / 2560, i = gt % 2560;
    const int unit = i >> 2, dd = i & 3, kb = unit >> 6, l = unit & 63, r16 = l & 15, g = l >> 4;
    const int col = 64 * kb + 16 * g + 4 * dd + 2 * (r16 >> 3);
    const u32 v = nosent(*(const u32 *)(p.mem0 + (r16 & 7) * D + col));
    const u32 in = (u32)i * 4;
    *(u32 *)(p.ho + ((size_t)(MBLK + (in >> 13)) << 14) + par * 8192 + (in & 8191)) = v;
  }
}

constexpr u32 LDS_BYTES = 160 * 1024;
__global__ void __launch_bounds__(256, 1) k_main(Params p) {
  extern __shared__ __attribute__((aligned(16))) unsigned char smem[];
  LAS unsigned char *L = (LAS unsigned char *)smem;
  const int b = blockIdx.x;
  const int ci = (b & 7) < 4 ? (b >> 3) * 4 + (b & 3) : NCHAIN;
  if (ci >= NS + NV && ci - (NS + NV) < NPAGE) probe_page(p, ci - (NS + NV));
  if (ci < NS) role_S(p, L, ci);
  else if (ci < NS + NV) role_V(p, L, ci - NS);
  else if (ci < NCHAIN) role_O(p, L, ci - NS - NV);
  else role_W(p, L, (b & 7) >= 4 ? (b & 7) - 4 : 4, (b & 7) >= 4 ? b >> 3 : ci - NCHAIN);
}

}

extern "C" void kernel_launch(void *const *d_in, const int *in_sizes, int n_in, void *d_out, int out_size, void *d_ws, size_t ws_size,
                              hipStream_t stream) {
  Params p;
  p.x = (const u16 *)d_in[0];
  p.mem0 = (const u16 *)d_in[1];
  p.Wq = (const u16 *)d_in[2];
  p.Wk = (const u16 *)d_in[3];
  p.Wv = (const u16 *)d_in[4];
  p.Wo = (const u16 *)d_in[5];
  p.Wg = (const u16 *)d_in[8];
  p.Wu = (const u16 *)d_in[9];
  p.Wd = (const u16 *)d_in[10];
  p.out = (u16 *)d_out;
  p.ws = (unsigned char *)d_ws;
  p.ho = (unsigned char *)(((uintptr_t)d_ws + OFF_HO + (2 * MB - 1)) & ~(uintptr_t)(2 * MB - 1));
  if (ws_size < WS_NEED) return;
  (void)hipFuncSetAttribute((const void *)k_main, hipFuncAttributeMaxDynamicSharedMemorySize, LDS_BYTES);
  k_pre<<<256, 256, 0, stream>>>(p);
  k_main<<<256, 256, LDS_BYTES, stream>>>(p);
}
```
